```python
import torch

def _softmax(x, axis=-1):
    return torch.softmax(x, dim=axis)
def _gelu(x):
    return torch.nn.functional.gelu(x, approximate="tanh")
def _silu(x):
    return torch.nn.functional.silu(x)
def _sigmoid(x):
    return torch.sigmoid(x)
def _layernorm(x, axis=-1, eps=1e-5):
    m = x.mean(dim=axis, keepdim=True)
    v = x.var(dim=axis, keepdim=True, unbiased=False)
    return (x - m) / torch.sqrt(v + eps)
def _scatter_add(tgt, idx, src, axis=0):
    return tgt.clone().index_add_(axis, idx, src)
def _segment_sum(src, seg, n):
    out = torch.zeros((n,) + src.shape[1:], dtype=src.dtype)
    return out.index_add_(0, seg, src)
def _pad(x, pw):
    flat = tuple(v for pair in reversed(pw) for v in pair)
    return torch.nn.functional.pad(x, flat)

kernel_name = 'sequential_preLN__mha_block_cached__geglu_ffn__decode'

def setup_inputs():

    _w = (lambda a: (lambda t: t.to(torch.bfloat16) if t.is_floating_point() else t)(torch.as_tensor(a)))
    def _A_setup():
        B, Sq, D = 8, 1, 640
        H, Sk, Dh = 32, 4096, 128
        return {
            "x": _w((lambda *s: torch.randn(*s, dtype=torch.bfloat16) * 0.05)(B, Sq, D)),
            "k_cache": _w((lambda *s: torch.randn(*s, dtype=torch.bfloat16) * 3.2)(B, H, Sk, Dh)),
            "v_cache": _w((lambda *s: torch.randn(*s, dtype=torch.bfloat16) * 0.4)(B, H, Sk, Dh)),
            "wq": _w((lambda *s: torch.randn(*s, dtype=torch.bfloat16) * 3.2)(D, H * Dh) * (D ** -0.5)),
            "wo": _w((lambda *s: torch.randn(*s, dtype=torch.bfloat16) * 0.4)(H * Dh, D) * ((H * Dh) ** -0.5)),
        }
    _A_in = _A_setup()

    def _B_setup():
        B, S, D, F = 8, 1, 640, 1728
        return {
            "x": _w((lambda *s: torch.randn(*s, dtype=torch.bfloat16) * 0.05)(B, S, D)),
            "w_gate": _w((lambda *s: torch.randn(*s, dtype=torch.bfloat16) * 0.05)(D, F)),
            "w_up": _w((lambda *s: torch.randn(*s, dtype=torch.bfloat16) * 0.05)(D, F)),
            "w_down": _w((lambda *s: torch.randn(*s, dtype=torch.bfloat16) * 0.05)(F, D)),
        }
    _B_in = _B_setup()

    return {
        "A_x": _A_in["x"],
        "A_k_cache": _A_in["k_cache"],
        "A_v_cache": _A_in["v_cache"],
        "A_wq": _A_in["wq"],
        "A_wo": _A_in["wo"],
        "B_x": _B_in["x"],
        "B_w_gate": _B_in["w_gate"],
        "B_w_up": _B_in["w_up"],
        "B_w_down": _B_in["w_down"],
    }

def reference(A_x, A_k_cache, A_v_cache, A_wq, A_wo, B_x, B_w_gate, B_w_up, B_w_down):
    def _A_ref(x, k_cache, v_cache, wq, wo):
        B, Sq, D = x.shape
        H, Dh = 32, 128
        q = (lambda x, *d: x.permute(*d))((x @ wq).reshape(B, Sq, H, Dh), 0, 2, 1, 3)
        scores = torch.einsum("bhqd,bhkd->bhqk", q, k_cache) * (Dh ** -0.5)
        p = _softmax(scores, axis=-1)
        o = torch.einsum("bhqk,bhkd->bhqd", p, v_cache)
        return (lambda x, *d: x.permute(*d))(o, 0, 2, 1, 3).reshape(B, Sq, H * Dh) @ wo

    def _B_ref(x, w_gate, w_up, w_down):
        return (_gelu(x @ w_gate) * (x @ w_up)) @ w_down

    x = A_x
    h = x + _A_ref(_layernorm(x, axis=-1), A_k_cache, A_v_cache, A_wq, A_wo)
    h2 = h + _B_ref(_layernorm(h, axis=-1), B_w_gate, B_w_up, B_w_down)
    return h2
```

```cpp
#include <hip/hip_runtime.h>
#include <hip/hip_bf16.h>
#include <stdint.h>

#define KTS 0
#define THR_D 6
#define NHG 2
#define WAUX 0

namespace {

typedef unsigned short u16;
typedef unsigned v4u __attribute__((ext_vector_type(4)));
typedef unsigned v2u __attribute__((ext_vector_type(2)));
typedef float f32x4 __attribute__((ext_vector_type(4)));
typedef float f32x2 __attribute__((ext_vector_type(2)));
typedef short s16x4 __attribute__((ext_vector_type(4)));
typedef short s16x8 __attribute__((ext_vector_type(8)));
typedef __bf16 bf16x2 __attribute__((ext_vector_type(2)));
typedef __bf16 bf16x8 __attribute__((ext_vector_type(8)));
#define LAS __attribute__((address_space(3)))
typedef __amdgpu_buffer_rsrc_t rsrc_t;

constexpr int DM = 640, NHEAD = 32, SK = 4096, DH = 128, FF = 1728;
constexpr int NTHR = 768;
constexpr int AUX_NT = 2, AUX_SC1 = 16;
constexpr unsigned SPIN_CAP = 1u << 17;

constexpr int WS_P = 4096;
constexpr int WS_H = WS_P + 8 * 32 * DM * 8;
constexpr int WS_A = WS_H + 8 * 320 * 8;
constexpr int WS_END = WS_A + 216 * 256;
constexpr int WS_TS = 2 << 20;

constexpr int L_QP = 0;
constexpr int L_OP = L_QP + 4096;
constexpr int L_SH = L_OP + 2048;
constexpr int L_FL = L_SH + 2048;
constexpr int L_XB = L_FL + 256;
constexpr int L_HN = L_XB + 12 * 256;
constexpr int HN_STRIDE = 1344;
constexpr int L_ST = L_HN + 8 * HN_STRIDE;
constexpr int L_FW = L_ST + 8 * 5120;
constexpr int L_END = L_FW + FF * 32;
static_assert(L_END <= 160 * 1024 && L_END > 80 * 1024, "one workgroup per CU");

struct Params {
  const u16 *x, *k, *v, *wq, *wo, *wg, *wu, *wd;
  u16* out;
  unsigned char* ws;
};

__device__ __forceinline__ float asf(unsigned u) { return __builtin_bit_cast(float, u); }
__device__ __forceinline__ unsigned asu(float f) { return __builtin_bit_cast(unsigned, f); }
__device__ __forceinline__ float dot2(unsigned a, unsigned b, float c) {
  return __builtin_amdgcn_fdot2_f32_bf16(__builtin_bit_cast(bf16x2, a), __builtin_bit_cast(bf16x2, b), c, false);
}
__device__ __forceinline__ unsigned pkbf(float lo, float hi) {
  bf16x2 z = {(__bf16)lo, (__bf16)hi};
  return __builtin_bit_cast(unsigned, z);
}
__device__ __forceinline__ float rbf(float f) { return asf((unsigned)__builtin_bit_cast(u16, (__bf16)f) << 16); }
__device__ __forceinline__ float blo(unsigned u) { return asf(u << 16); }
__device__ __forceinline__ float bhi(unsigned u) { return asf(u & 0xffff0000u); }
template <int CTRL> __device__ __forceinline__ float dppf(float x) {
  return asf((unsigned)__builtin_amdgcn_mov_dpp((int)asu(x), CTRL, 0xf, 0xf, true));
}
template <int CTRL> __device__ __forceinline__ unsigned dppu(unsigned x) {
  return (unsigned)__builtin_amdgcn_mov_dpp((int)x, CTRL, 0xf, 0xf, true);
}
constexpr int XOR1 = 0xB1, XOR2 = 0x4E, XOR7 = 0x141, XOR8 = 0x128;
__device__ __forceinline__ float swap32_add(float a, float b) {
  auto s = __builtin_amdgcn_permlane32_swap(asu(a), asu(b), false, false);
  return asf(s[0]) + asf(s[1]);
}
__device__ __forceinline__ float swap16_add(float a, float b) {
  auto s = __builtin_amdgcn_permlane16_swap(asu(a), asu(b), false, false);
  return asf(s[0]) + asf(s[1]);
}
__device__ __forceinline__ float wave_sum(float x) {
  x += dppf<XOR1>(x); x += dppf<XOR2>(x); x += dppf<XOR7>(x); x += dppf<XOR8>(x);
  x = swap16_add(x, x);
  return swap32_add(x, x);
}
__device__ __forceinline__ float wave_max(float x) {
  x = fmaxf(x, dppf<XOR1>(x)); x = fmaxf(x, dppf<XOR2>(x)); x = fmaxf(x, dppf<XOR7>(x)); x = fmaxf(x, dppf<XOR8>(x));
  auto s = __builtin_amdgcn_permlane16_swap(asu(x), asu(x), false, false);
  x = fmaxf(asf(s[0]), asf(s[1]));
  auto t = __builtin_amdgcn_permlane32_swap(asu(x), asu(x), false, false);
  return fmaxf(asf(t[0]), asf(t[1]));
}
__device__ __forceinline__ rsrc_t mkrsrc(const void* p, unsigned bytes) {
  return __builtin_amdgcn_make_buffer_rsrc((void*)p, 0, (int)bytes, 0x00020000);
}
template <int AUX> __device__ __forceinline__ v4u ld16(rsrc_t r, int voff, int soff) {
  return __builtin_amdgcn_raw_buffer_load_b128(r, voff, soff, AUX);
}
__device__ __forceinline__ f32x4 mfma16(v4u a, v4u b, f32x4 c) {
  return __builtin_amdgcn_mfma_f32_16x16x32_bf16(__builtin_bit_cast(bf16x8, a), __builtin_bit_cast(bf16x8, b), c, 0, 0, 0);
}
__device__ __forceinline__ v4u tr_frag(LAS unsigned char* p, int half_stride) {
  const v2u a = __builtin_bit_cast(v2u, __builtin_amdgcn_ds_read_tr16_b64_v4i16((LAS s16x4*)p));
  const v2u b = __builtin_bit_cast(v2u, __builtin_amdgcn_ds_read_tr16_b64_v4i16((LAS s16x4*)(p + half_stride)));
  return v4u{a.x, a.y, b.x, b.y};
}
#define SB() __builtin_amdgcn_sched_barrier(0)
#define LDS_WAIT() asm volatile("s_waitcnt lgkmcnt(0)" ::: "memory")
#define BARRIER() do { asm volatile("s_waitcnt lgkmcnt(0)\n\ts_barrier" ::: "memory"); } while (0)
#define STR2(x) #x
#define STR(x) STR2(x)
#define THROTTLE() asm volatile("s_waitcnt vmcnt(%0)" ::"n"(THR_D - 1))
constexpr int brev4(int a) { return ((a & 1) << 3) | ((a & 2) << 1) | ((a & 4) >> 1) | ((a & 8) >> 3); }

#define STAMP(i) do {} while (0)

__device__ __forceinline__ void layernorm5(unsigned (&u)[5]) {
  float a[10];
#pragma unroll
  for (int i = 0; i < 5; ++i) { a[2 * i] = blo(u[i]); a[2 * i + 1] = bhi(u[i]); }
  float s = 0.f;
#pragma unroll
  for (int i = 0; i < 10; ++i) s += a[i];
  const float mean = wave_sum(s) * (1.f / DM);
  float vs = 0.f;
#pragma unroll
  for (int i = 0; i < 10; ++i) { const float d = a[i] - mean; vs = fmaf(d, d, vs); }
  const float var = wave_sum(vs) * (1.f / DM);
  const float mb = rbf(mean), sd = rbf(__builtin_sqrtf(rbf(rbf(var) + 1e-5f)));
  const float inv = 1.f / sd;
#pragma unroll
  for (int i = 0; i < 5; ++i) u[i] = pkbf(rbf(a[2 * i] - mb) * inv, rbf(a[2 * i + 1] - mb) * inv);
}

template <int CTRL> __device__ __forceinline__ float tr_step(float lo, float hi, bool bit) {
  const float t1 = lo + dppf<CTRL>(lo), t2 = hi + dppf<CTRL>(hi);
  return bit ? t2 : t1;
}
__device__ __forceinline__ float qk4(const v4u& kk, const v4u& q) {
  return dot2(kk.w, q.w, dot2(kk.z, q.z, dot2(kk.y, q.y, dot2(kk.x, q.x, 0.f))));
}
__device__ __forceinline__ float score_round(float x) { return rbf(rbf(x) * 0.08838834764831845f); }

struct TrAcc {
  float l0, l1, l2, l3;
  template <int A> __device__ __forceinline__ void push(float x, int c) {
    if (!(A & 1)) { l0 = x; return; }
    x = tr_step<XOR8>(l0, x, c & 8);
    if (!(A & 2)) { l1 = x; return; }
    x = tr_step<XOR7>(l1, x, c & 4);
    if (!(A & 4)) { l2 = x; return; }
    x = tr_step<XOR2>(l2, x, c & 2);
    if (!(A & 8)) { l3 = x; return; }
    l0 = tr_step<XOR1>(l3, x, c & 1);
  }
};
template <int I, int N, class F> __device__ __forceinline__ void sfor(F&& f) {
  if constexpr (I < N) { f(std::integral_constant<int, I>{}); sfor<I + 1, N>(f); }
}

__device__ __forceinline__ v4u build_q(LAS unsigned char* L, int wave, int lane) {
  f32x2 s = *(LAS f32x2*)(L + L_QP + lane * 8);
#pragma unroll
  for (int i = 1; i < 8; ++i) s += *(LAS f32x2*)(L + L_QP + i * 512 + lane * 8);
  *(LAS unsigned*)(L + L_XB + wave * 256 + lane * 4) = pkbf(s.x, s.y);
  LDS_WAIT();
  return *(LAS v4u*)(L + L_XB + wave * 256 + (lane & 15) * 16);
}

__global__ void __launch_bounds__(NTHR) fused_block(Params p) {
  __shared__ __attribute__((aligned(16))) unsigned char smem[L_END];
  LAS unsigned char* L = (LAS unsigned char*)smem;
  const int tid = threadIdx.x, lane = tid & 63, wave = __builtin_amdgcn_readfirstlane(tid >> 6);
  const int r = lane >> 4, c = lane & 15;
  const int xc = blockIdx.x & 7, sl = blockIdx.x >> 3;
  const int h = xc + 8 * (sl & 3), b = sl >> 2;
  const int g2 = xc + 8 * (sl >> 3);
  const bool is_t2 = g2 < 27;
  const int j2 = 8 * g2 + (sl & 7);
  const int t3 = 8 * (xc - 3) + (sl & 7);
  const size_t kvoff = (size_t)(b * NHEAD + h) * (SK * DH);
  const rsrc_t rK = mkrsrc(p.k + kvoff, SK * DH * 2);
  const rsrc_t rW = mkrsrc(p.ws, WS_END);
  volatile LAS unsigned* FL = (volatile LAS unsigned*)(L + L_FL);
  STAMP(0);

  if (wave < 4) {
    const rsrc_t rV = mkrsrc(p.v + kvoff, SK * DH * 2);
    const int voff = lane * 16 + wave * 4096;
    v4u buf[8];
    sfor<0, 8>([&](auto T) { constexpr int t = T;
      if (t >= THR_D) THROTTLE();
      buf[t] = ld16<AUX_NT>(rK, voff + (t & 3) * 1024, NHG * 65536 + (t >> 2) * 16384); SB(); });
    unsigned xres = 0;
    if (wave == 0 && h < 20) xres = ((const unsigned*)p.x)[b * 320 + 16 * h + c];
    BARRIER();
    STAMP(1);
    const unsigned tag = FL[0];
    const v4u q = build_q(L, wave, lane);
    float s[16];
#pragma unroll
    for (int i = 0; i < 16; ++i) s[i] = 0.f;
#pragma unroll 1
    for (int g = NHG; g < 16; ++g) {
      const bool last = g == 15;
      const rsrc_t rN = last ? rV : rK;
      const int so0 = g * 65536 + 32768, so1 = last ? 0 : (g + 1) * 65536;
      TrAcc ta;
      sfor<0, 16>([&](auto T) { constexpr int t = T;
        const float x = qk4(buf[t & 7], q);
        SB();
        THROTTLE();
        if (t < 8) buf[t & 7] = ld16<AUX_NT>(rK, voff + (t & 3) * 1024, so0 + (t >> 2) * 16384);
        else buf[t & 7] = ld16<AUX_NT>(rN, voff + (t & 3) * 1024, so1 + ((t - 8) >> 2) * 16384);
        SB();
        ta.push<t>(x, c); });
#pragma unroll
      for (int i = 0; i < 15; ++i) s[i] = s[i + 1];
      s[15] = score_round(ta.l0);
    }
    STAMP(2);
#pragma unroll
    for (int g = 0; g < NHG; ++g) {
      for (unsigned spin = 0; FL[4 + 4 * g + wave] == 0u && spin < SPIN_CAP; ++spin) __builtin_amdgcn_s_sleep(1);
      s[g] = *(volatile LAS float*)(L + L_SH + (4 * g + wave) * 256 + lane * 4);
    }
    float m = s[0];
#pragma unroll
    for (int i = 1; i < 16; ++i) m = fmaxf(m, s[i]);
    m = wave_max(m);
    float l = 0.f;
    unsigned pp[16];
#pragma unroll
    for (int i = 0; i < 16; ++i) {
      const float e = __builtin_amdgcn_exp2f((s[i] - m) * 1.4426950408889634f);
      l += e;
      pp[i] = (unsigned)__builtin_bit_cast(u16, (__bf16)e);
    }
    l = wave_sum(l);
    if (lane == 0) {
      *(volatile LAS f32x2*)(L + L_FL + 128 + wave * 8) = f32x2{m, l};
      LDS_WAIT();
      FL[16 + wave] = 1u;
    }
    float acc[8];
#pragma unroll
    for (int i = 0; i < 8; ++i) acc[i] = 0.f;
    float scale = 0.f;
#pragma unroll 1
    for (int g = 0; g < 16; ++g) {
      const int so0 = g * 65536 + 32768, so1 = (g + 1) * 65536;
      const unsigned pc = pp[0];
#pragma unroll
      for (int i = 0; i < 15; ++i) pp[i] = pp[i + 1];
      if (g == 15) {
        for (unsigned spin = 0; (FL[16] & FL[17] & FL[18] & FL[19]) == 0u && spin < SPIN_CAP; ++spin) __builtin_amdgcn_s_sleep(1);
        const f32x4 s01 = *(volatile LAS f32x4*)(L + L_FL + 128), s23 = *(volatile LAS f32x4*)(L + L_FL + 144);
        const float mg = fmaxf(fmaxf(s01.x, s01.z), fmaxf(s23.x, s23.z));
        constexpr float LE = 1.4426950408889634f;
        const float lg = s01.y * __builtin_amdgcn_exp2f((s01.x - mg) * LE) + s01.w * __builtin_amdgcn_exp2f((s01.z - mg) * LE) +
                         s23.y * __builtin_amdgcn_exp2f((s23.x - mg) * LE) + s23.w * __builtin_amdgcn_exp2f((s23.z - mg) * LE);
        scale = __builtin_amdgcn_exp2f((m - mg) * LE) / lg;
      }
      sfor<0, 16>([&](auto T) { constexpr int t = T;
        const unsigned plo = dppu<0x150 + brev4(t)>(pc), phi = plo << 16;
        const v4u vv = buf[t & 7];
        acc[0] = dot2(vv.x, plo, acc[0]); acc[1] = dot2(vv.x, phi, acc[1]);
        acc[2] = dot2(vv.y, plo, acc[2]); acc[3] = dot2(vv.y, phi, acc[3]);
        acc[4] = dot2(vv.z, plo, acc[4]); acc[5] = dot2(vv.z, phi, acc[5]);
        acc[6] = dot2(vv.w, plo, acc[6]); acc[7] = dot2(vv.w, phi, acc[7]);
        SB();
        THROTTLE();
        if (t < 8) buf[t & 7] = ld16<AUX_NT>(rV, voff + (t & 3) * 1024, so0 + (t >> 2) * 16384);
        else buf[t & 7] = ld16<AUX_NT>(rV, voff + (t & 3) * 1024, so1 + ((t - 8) >> 2) * 16384);
        SB(); });
    }
    {
      const float y0 = swap32_add(acc[0], acc[4]), y1 = swap32_add(acc[1], acc[5]), y2 = swap32_add(acc[2], acc[6]), y3 = swap32_add(acc[3], acc[7]);
      const float z0 = swap16_add(y0, y2), z1 = swap16_add(y1, y3);
      *(LAS f32x2*)(L + L_OP + wave * 512 + (8 * c + 2 * r) * 4) = f32x2{z0 * scale, z1 * scale};
    }
    STAMP(3);
    BARRIER();
    if (wave == 0) {
      if (h < 20) {
        const int po = WS_P + (b * NHEAD + r) * (DM * 8) + h * 256 + c * 16;
        v4u g[8];
        for (unsigned spin = 0;; ++spin) {
          bool ok = true;
#pragma unroll
          for (int i = 0; i < 8; ++i) g[i] = ld16<AUX_SC1>(rW, po, i * (4 * DM * 8));
#pragma unroll
          for (int i = 0; i < 8; ++i) ok &= (g[i].y == tag) & (g[i].w == tag);
          if (__all(ok) || spin > SPIN_CAP) break;
          asm volatile("s_sleep 1" ::: "memory");
        }
        float a0 = 0.f, a1 = 0.f;
#pragma unroll
        for (int i = 0; i < 8; ++i) { a0 += asf(g[i].x); a1 += asf(g[i].z); }
        a0 = swap16_add(a0, a0); a1 = swap16_add(a1, a1);
        a0 = swap32_add(a0, a0); a1 = swap32_add(a1, a1);
        const unsigned hv = pkbf(blo(xres) + rbf(a0), bhi(xres) + rbf(a1));
        if (r == 0) __builtin_amdgcn_raw_buffer_store_b64(v2u{hv, tag}, rW, WS_H + (b * 320 + 16 * h + c) * 8, 0, AUX_SC1);
        STAMP(4);
      }
    } else if (wave == 1 && is_t2) {
      v4u wf[20];
      LAS unsigned char* wp = L + L_FW + (8 * r + (c >> 2)) * 32 + (c & 3) * 8;
#pragma unroll
      for (int ks = 0; ks < 20; ++ks) wf[ks] = tr_frag(wp + ks * 1024, 128);
      BARRIER();
      STAMP(5);
      f32x4 d0 = {0.f, 0.f, 0.f, 0.f}, d1 = d0;
      LAS unsigned char* hp = L + L_HN + (c & 7) * HN_STRIDE + r * 16;
#pragma unroll
      for (int ks = 0; ks < 20; ks += 2) {
        d0 = mfma16(wf[ks], *(LAS v4u*)(hp + ks * 64), d0);
        d1 = mfma16(wf[ks + 1], *(LAS v4u*)(hp + ks * 64 + 64), d1);
      }
      d0 += d1;
      float av[2];
#pragma unroll
      for (int i = 0; i < 2; ++i) {
        const float g = rbf(d0[i]), u = rbf(d0[2 + i]);
        const float y = 0.7978845608028654f * (g + 0.044715f * g * g * g);
        const float th = 1.f - 2.f / (__builtin_amdgcn_exp2f(y * 2.8853900817779268f) + 1.f);
        av[i] = rbf(0.5f * g * (1.f + th)) * u;
      }
      if (c < 8) __builtin_amdgcn_raw_buffer_store_b64(v2u{pkbf(av[0], av[1]), tag}, rW, WS_A + j2 * 256 + c * 32 + r * 8, 0, AUX_SC1);
      STAMP(6);
    }
    return;
  }

  const int hw = wave - 4;
  v4u wb[20];
  {
    unsigned xr[5];
#pragma unroll
    for (int i = 0; i < 5; ++i) xr[i] = ((const unsigned*)p.x)[b * 320 + lane + 64 * i];
    const rsrc_t rQ = mkrsrc(p.wq, DM * 4096 * 2);
    const int qoff = (80 * hw + 20 * r) * 8192 + h * 256 + c * 16;
#pragma unroll
    for (int t = 0; t < 20; ++t) wb[t] = ld16<WAUX>(rQ, qoff, t * 8192);
    if (hw == 0) {
      const unsigned e = *(const unsigned*)p.ws;
      if (lane < 24) FL[lane] = lane == 0 ? e + 1u : 0u;
    }
    SB();
    layernorm5(xr);
    LAS unsigned char* xp = L + L_HN + hw * HN_STRIDE;
#pragma unroll
    for (int i = 0; i < 5; ++i) *(LAS unsigned*)(xp + (lane + 64 * i) * 4) = xr[i];
    LDS_WAIT();
    unsigned xn[10];
#pragma unroll
    for (int i = 0; i < 5; ++i) { const v2u t = *(LAS v2u*)(xp + (80 * hw + 20 * r) * 2 + i * 8); xn[2 * i] = t.x; xn[2 * i + 1] = t.y; }
    float acc[8];
#pragma unroll
    for (int i = 0; i < 8; ++i) acc[i] = 0.f;
    const int koff = lane * 16 + (hw & 3) * 4096;
    sfor<0, 20>([&](auto T) { constexpr int t = T;
      const unsigned u = xn[t >> 1];
      const unsigned plo = (t & 1) ? u >> 16 : u & 0xffffu, phi = (t & 1) ? u & 0xffff0000u : u << 16;
      const v4u vv = wb[t];
      acc[0] = dot2(vv.x, plo, acc[0]); acc[1] = dot2(vv.x, phi, acc[1]);
      acc[2] = dot2(vv.y, plo, acc[2]); acc[3] = dot2(vv.y, phi, acc[3]);
      acc[4] = dot2(vv.z, plo, acc[4]); acc[5] = dot2(vv.z, phi, acc[5]);
      acc[6] = dot2(vv.w, plo, acc[6]); acc[7] = dot2(vv.w, phi, acc[7]);
      SB();
      if (NHG > 0 && t < 16) wb[t] = ld16<AUX_NT>(rK, koff + (t & 3) * 1024, (hw < 4 * NHG ? (hw >> 2) * 65536 : SK * DH * 2) + (t >> 2) * 16384);
      SB(); });
    const float y0 = swap32_add(acc[0], acc[4]), y1 = swap32_add(acc[1], acc[5]), y2 = swap32_add(acc[2], acc[6]), y3 = swap32_add(acc[3], acc[7]);
    const float z0 = swap16_add(y0, y2), z1 = swap16_add(y1, y3);
    *(LAS f32x2*)(L + L_QP + hw * 512 + (8 * c + 2 * r) * 4) = f32x2{z0, z1};
  }
  BARRIER();
  STAMP(1);
  const unsigned tag = FL[0];
  if (NHG > 0 && hw < 4 * NHG) {
    const v4u q = build_q(L, wave, lane);
    TrAcc ta;
    sfor<0, 16>([&](auto T) { constexpr int t = T; ta.push<t>(qk4(wb[t], q), c); });
    *(LAS float*)(L + L_SH + hw * 256 + lane * 4) = score_round(ta.l0);
    LDS_WAIT();
    if (lane == 0) FL[4 + hw] = 1u;
  }
  STAMP(2);
  {
    const rsrc_t rO = mkrsrc(p.wo + (size_t)h * DH * DM, DH * DM * 2);
    LAS unsigned char* st = L + L_ST + hw * 5120;
    LAS unsigned char* rp = st + (8 * r + (c >> 2)) * 160 + (c & 3) * 8;
#pragma unroll
    for (int ks = 0; ks < 4; ++ks) {
      v4u t[5];
#pragma unroll
      for (int i = 0; i < 5; ++i) { const int ch = 64 * i + lane, row = ch / 10; t[i] = ld16<WAUX>(rO, row * 1280 + (ch - row * 10) * 16 + hw * 160, ks * 32 * 1280); }
#pragma unroll
      for (int i = 0; i < 5; ++i) *(LAS v4u*)(st + (64 * i + lane) * 16) = t[i];
      LDS_WAIT();
#pragma unroll
      for (int tt = 0; tt < 5; ++tt) wb[ks * 5 + tt] = tr_frag(rp + tt * 32, 640);
      LDS_WAIT();
    }
  }
  {
    const int ht = tid - 256;
    if (is_t2) {
      const rsrc_t rG = mkrsrc(p.wg, DM * FF * 2), rU = mkrsrc(p.wu, DM * FF * 2);
#pragma unroll 1
      for (int row = ht & 255; row < DM; row += 256) {
        const v4u t = hw < 4 ? ld16<WAUX>(rG, row * (FF * 2) + j2 * 16, 0) : ld16<WAUX>(rU, row * (FF * 2) + j2 * 16, 0);
        LAS unsigned* d = (LAS unsigned*)(L + L_FW + row * 32 + (hw >> 2) * 4);
        d[0] = t.x; d[2] = t.y; d[4] = t.z; d[6] = t.w;
      }
    } else {
      const rsrc_t rD = mkrsrc(p.wd, FF * DM * 2);
#pragma unroll 1
      for (int i = ht; i < FF * 2; i += 512) *(LAS v4u*)(L + L_FW + i * 16) = ld16<WAUX>(rD, (i >> 1) * (DM * 2) + t3 * 32 + (i & 1) * 16, 0);
    }
  }
  STAMP(3);
  BARRIER();
  STAMP(4);
  {
    f32x2 o2 = *(LAS f32x2*)(L + L_OP + lane * 8);
#pragma unroll
    for (int i = 1; i < 4; ++i) o2 += *(LAS f32x2*)(L + L_OP + i * 512 + lane * 8);
    *(LAS unsigned*)(L + L_XB + wave * 256 + lane * 4) = pkbf(o2.x, o2.y);
    LDS_WAIT();
    v4u oa[4];
#pragma unroll
    for (int ks = 0; ks < 4; ++ks) oa[ks] = *(LAS v4u*)(L + L_XB + wave * 256 + ks * 64 + r * 16);
    float d[5], e[5];
#pragma unroll
    for (int tt = 0; tt < 5; ++tt) {
      f32x4 a = {0.f, 0.f, 0.f, 0.f};
#pragma unroll
      for (int ks = 0; ks < 4; ++ks) a = mfma16(oa[ks], wb[ks * 5 + tt], a);
      d[tt] = a.x; e[tt] = dppf<XOR1>(a.x);
    }
    const float v0 = r == 0 ? d[0] : r == 1 ? d[1] : r == 2 ? d[2] : d[3];
    const float v1 = r == 0 ? e[0] : r == 1 ? e[1] : r == 2 ? e[2] : e[3];
    const bool odd = c & 1;
    const int col = 80 * hw + (odd ? 64 + c - 1 : lane);
    if (!odd || r == 0)
      __builtin_amdgcn_raw_buffer_store_b128(v4u{asu(odd ? e[4] : v0), tag, asu(odd ? d[4] : v1), tag}, rW, WS_P + ((b * NHEAD + h) * DM + col) * 8, 0, AUX_SC1);
  }
  STAMP(5);
  if (is_t2) {
    unsigned hr[5];
    for (unsigned spin = 0;; ++spin) {
      v2u g[5];
      bool ok = true;
#pragma unroll
      for (int i = 0; i < 5; ++i) g[i] = __builtin_amdgcn_raw_buffer_load_b64(rW, WS_H + (hw * 320 + lane) * 8, i * 512, AUX_SC1);
#pragma unroll
      for (int i = 0; i < 5; ++i) { ok &= g[i].y == tag; hr[i] = g[i].x; }
      if (__all(ok) || spin > SPIN_CAP) break;
      asm volatile("s_sleep 1" ::: "memory");
    }
    STAMP(6);
    layernorm5(hr);
#pragma unroll
    for (int i = 0; i < 5; ++i) *(LAS unsigned*)(L + L_HN + hw * HN_STRIDE + (lane + 64 * i) * 4) = hr[i];
    BARRIER();
  } else {
    v4u wf[7];
    LAS unsigned char* wp = L + L_FW + (8 * r + (c >> 2)) * 32 + (c & 3) * 8;
#pragma unroll
    for (int i = 0; i < 7; ++i) { const int ks = min(7 * hw + i, 53); wf[i] = tr_frag(wp + ks * 1024, 128); }
    v4u hres = {0u, 0u, 0u, 0u};
    if (hw == 0) {
      for (unsigned spin = 0;; ++spin) {
        hres = ld16<AUX_SC1>(rW, WS_H + ((c & 7) * 320 + 8 * t3 + 2 * r) * 8, 0);
        if (__all(hres.y == tag && hres.w == tag) || spin > SPIN_CAP) break;
        asm volatile("s_sleep 1" ::: "memory");
      }
    }
    v4u g[7];
    const int ao = WS_A + r * 256 + (c & 7) * 32 + (c >> 3) * 16;
    const int nks = hw == 7 ? 5 : 7;
    for (unsigned spin = 0;; ++spin) {
      bool ok = true;
#pragma unroll
      for (int i = 0; i < 7; ++i) g[i] = ld16<AUX_SC1>(rW, ao, min(7 * hw + i, 53) * 1024);
#pragma unroll
      for (int i = 0; i < 7; ++i) ok &= (g[i].y == tag) & (g[i].w == tag);
      if (__all(ok) || spin > SPIN_CAP) break;
      asm volatile("s_sleep 1" ::: "memory");
    }
    STAMP(6);
    f32x4 a = {0.f, 0.f, 0.f, 0.f};
#pragma unroll
    for (int i = 0; i < 7; ++i) {
      v4u bf = {g[i].x, g[i].z, dppu<XOR8>(g[i].x), dppu<XOR8>(g[i].z)};
      if (i >= 5 && i >= nks) bf = v4u{0u, 0u, 0u, 0u};
      a = mfma16(wf[i], bf, a);
    }
    *(LAS f32x4*)(L + L_ST + hw * 1024 + lane * 16) = a;
    BARRIER();
    if (hw == 0) {
      f32x4 t = *(LAS f32x4*)(L + L_ST + lane * 16);
#pragma unroll
      for (int i = 1; i < 8; ++i) t += *(LAS f32x4*)(L + L_ST + i * 1024 + lane * 16);
      const v2u o = {pkbf(blo(hres.x) + rbf(t.x), bhi(hres.x) + rbf(t.y)), pkbf(blo(hres.z) + rbf(t.z), bhi(hres.z) + rbf(t.w))};
      if (c < 8) *(v2u*)(p.out + c * DM + 16 * t3 + 4 * r) = o;
      if (t3 == 0 && lane == 0) __hip_atomic_store((unsigned*)p.ws, tag, __ATOMIC_RELAXED, __HIP_MEMORY_SCOPE_AGENT);
      STAMP(7);
    }
  }
}

}

extern "C" void kernel_launch(void* const* d_in, const int* in_sizes, int n_in, void* d_out, int out_size, void* d_ws,
                              size_t ws_size, hipStream_t stream) {
  Params p;
  p.x = (const u16*)d_in[0]; p.k = (const u16*)d_in[1]; p.v = (const u16*)d_in[2];
  p.wq = (const u16*)d_in[3]; p.wo = (const u16*)d_in[4];
  p.wg = (const u16*)d_in[6]; p.wu = (const u16*)d_in[7]; p.wd = (const u16*)d_in[8];
  p.out = (u16*)d_out; p.ws = (unsigned char*)d_ws;
  fused_block<<<256, NTHR, 0, stream>>>(p);
}
```

```cpp
#include <hip/hip_runtime.h>
#include <hip/hip_bf16.h>
#include <stdint.h>
#include <type_traits>

#define PFX 1
#define T3E 20
#define XM 0
#define KTS 0

namespace {

typedef unsigned short u16;
typedef unsigned v4u __attribute__((ext_vector_type(4)));
typedef unsigned v2u __attribute__((ext_vector_type(2)));
typedef float f32x4 __attribute__((ext_vector_type(4)));
typedef float f32x2 __attribute__((ext_vector_type(2)));
typedef short s16x4 __attribute__((ext_vector_type(4)));
typedef __bf16 bf16x2 __attribute__((ext_vector_type(2)));
typedef __bf16 bf16x8 __attribute__((ext_vector_type(8)));
#define LAS __attribute__((address_space(3)))
typedef __amdgpu_buffer_rsrc_t rsrc_t;

constexpr int DM = 640, NHEAD = 32, SK = 4096, DH = 128, FF = 1728;
constexpr int NTHR = 512;
constexpr int THR_D = 5;
constexpr int NXG = 3;
constexpr int QD = 4;
constexpr int AUX_NT = 2, AUX_SC1 = 16;
constexpr int OOB = 0x7ffffff0;
constexpr unsigned SPIN_CAP = 1u << 17;

constexpr int WS_F = 4096;
constexpr int WS_P = WS_F + 2048;
constexpr int WS_H = WS_P + 8 * 32 * DM * 4;
constexpr int WS_A = WS_H + 8 * DM * 2;
constexpr int WS_END = WS_A + 216 * 128;
constexpr unsigned EMPTY = 0xffffffffu;
constexpr int WS_TS = 8 << 20;

constexpr int L_QP = 0;
constexpr int L_OP = L_QP + 2048;
constexpr int L_FL = L_OP + 2048;
constexpr int L_XR = L_FL + 256;
constexpr int L_XB = L_XR + 1280;
constexpr int L_HN = L_XB + 8 * 256;
constexpr int HN_STRIDE = 1344;
constexpr int L_ST = L_HN + 8 * HN_STRIDE;
constexpr int L_FW = L_ST + 32 * 1280;
constexpr int L_END = L_FW + FF * 32;
static_assert(L_END <= 160 * 1024 && L_END > 80 * 1024, "one workgroup per CU");

struct Params {
  const u16 *x, *k, *v, *wq, *wo, *wg, *wu, *wd;
  u16* out;
  unsigned char* ws;
};

__device__ __forceinline__ float asf(unsigned u) { return __builtin_bit_cast(float, u); }
__device__ __forceinline__ unsigned asu(float f) { return __builtin_bit_cast(unsigned, f); }
__device__ __forceinline__ float dot2(unsigned a, unsigned b, float c) {
  return __builtin_amdgcn_fdot2_f32_bf16(__builtin_bit_cast(bf16x2, a), __builtin_bit_cast(bf16x2, b), c, false);
}
__device__ __forceinline__ unsigned pkbf(float lo, float hi) {
  bf16x2 z = {(__bf16)lo, (__bf16)hi};
  return __builtin_bit_cast(unsigned, z);
}
__device__ __forceinline__ float rbf(float f) { return asf((unsigned)__builtin_bit_cast(u16, (__bf16)f) << 16); }
__device__ __forceinline__ float blo(unsigned u) { return asf(u << 16); }
__device__ __forceinline__ float bhi(unsigned u) { return asf(u & 0xffff0000u); }
template <int CTRL> __device__ __forceinline__ float dppf(float x) {
  return asf((unsigned)__builtin_amdgcn_mov_dpp((int)asu(x), CTRL, 0xf, 0xf, true));
}
template <int CTRL> __device__ __forceinline__ unsigned dppu(unsigned x) {
  return (unsigned)__builtin_amdgcn_mov_dpp((int)x, CTRL, 0xf, 0xf, true);
}
constexpr int XOR1 = 0xB1, XOR2 = 0x4E, XOR7 = 0x141, XOR8 = 0x128;
__device__ __forceinline__ float swap32_add(float a, float b) {
  auto s = __builtin_amdgcn_permlane32_swap(asu(a), asu(b), false, false);
  return asf(s[0]) + asf(s[1]);
}
__device__ __forceinline__ float swap16_add(float a, float b) {
  auto s = __builtin_amdgcn_permlane16_swap(asu(a), asu(b), false, false);
  return asf(s[0]) + asf(s[1]);
}
__device__ __forceinline__ float wave_sum(float x) {
  x += dppf<XOR1>(x); x += dppf<XOR2>(x); x += dppf<XOR7>(x); x += dppf<XOR8>(x);
  x = swap16_add(x, x);
  return swap32_add(x, x);
}
__device__ __forceinline__ float wave_max(float x) {
  x = fmaxf(x, dppf<XOR1>(x)); x = fmaxf(x, dppf<XOR2>(x)); x = fmaxf(x, dppf<XOR7>(x)); x = fmaxf(x, dppf<XOR8>(x));
  auto s = __builtin_amdgcn_permlane16_swap(asu(x), asu(x), false, false);
  x = fmaxf(asf(s[0]), asf(s[1]));
  auto t = __builtin_amdgcn_permlane32_swap(asu(x), asu(x), false, false);
  return fmaxf(asf(t[0]), asf(t[1]));
}
__device__ __forceinline__ rsrc_t mkrsrc(const void* p, unsigned bytes) {
  return __builtin_amdgcn_make_buffer_rsrc((void*)p, 0, (int)bytes, 0x00020000);
}
template <int AUX> __device__ __forceinline__ v4u ld16(rsrc_t r, int voff, int soff) {
  return __builtin_amdgcn_raw_buffer_load_b128(r, voff, soff, AUX);
}
__device__ __forceinline__ f32x4 mfma16(v4u a, v4u b, f32x4 c) {
  return __builtin_amdgcn_mfma_f32_16x16x32_bf16(__builtin_bit_cast(bf16x8, a), __builtin_bit_cast(bf16x8, b), c, 0, 0, 0);
}
__device__ __forceinline__ v4u tr_frag(LAS unsigned char* p, int half_stride) {
  const v2u a = __builtin_bit_cast(v2u, __builtin_amdgcn_ds_read_tr16_b64_v4i16((LAS s16x4*)p));
  const v2u b = __builtin_bit_cast(v2u, __builtin_amdgcn_ds_read_tr16_b64_v4i16((LAS s16x4*)(p + half_stride)));
  return v4u{a.x, a.y, b.x, b.y};
}
#define SB() __builtin_amdgcn_sched_barrier(0)
#define PIN(x) asm volatile("" : "+v"(x))
#define LDS_WAIT() asm volatile("s_waitcnt lgkmcnt(0)" ::: "memory")
#define BARRIER() do { asm volatile("s_waitcnt lgkmcnt(0)\n\ts_barrier" ::: "memory"); } while (0)
#define THROTTLE() asm volatile("s_waitcnt vmcnt(%0)" ::"n"(THR_D - 1))
#define THROTTLE_X(e) asm volatile("s_waitcnt vmcnt(%1)\n\ts_cmp_lg_u32 %0, 0\n\ts_cbranch_scc1 .Le%=\n\ts_waitcnt vmcnt(%2)\n.Le%=:" ::"s"(e), "n"(THR_D), "n"(THR_D - 1) : "scc")
#define THROTTLE_IF(g) asm volatile("s_cmp_eq_u32 %0, 15\n\ts_cbranch_scc1 .Le%=\n\ts_waitcnt vmcnt(%1)\n.Le%=:" ::"s"(g), "n"(THR_D - 1) : "scc")
constexpr int brev4(int a) { return ((a & 1) << 3) | ((a & 2) << 1) | ((a & 4) >> 1) | ((a & 8) >> 3); }

#define STAMP(i) do {} while (0)
#define FLUSH() do {} while (0)

__device__ __forceinline__ void layernorm5(unsigned (&u)[5]) {
  float a[10];
#pragma unroll
  for (int i = 0; i < 5; ++i) { a[2 * i] = blo(u[i]); a[2 * i + 1] = bhi(u[i]); }
  const float piv = asf(__builtin_amdgcn_readfirstlane(asu(a[0])));
  f32x2 s2 = {0.f, 0.f}, q2 = s2;
#pragma unroll
  for (int i = 0; i < 5; ++i) { const f32x2 d = f32x2{a[2 * i], a[2 * i + 1]} - piv; s2 += d; q2 += d * d; }
  const float ss = q2.x + q2.y;
  const float ms = wave_sum(s2.x + s2.y) * (1.f / DM);
  const float var = fmaxf(fmaf(-ms, ms, wave_sum(ss) * (1.f / DM)), 0.f);
  const float mb = rbf(piv + ms), sd = rbf(__builtin_amdgcn_sqrtf(rbf(rbf(var) + 1e-5f)));
  const float inv = __builtin_amdgcn_rcpf(sd);
#pragma unroll
  for (int i = 0; i < 5; ++i) u[i] = pkbf(rbf(a[2 * i] - mb) * inv, rbf(a[2 * i + 1] - mb) * inv);
}

template <int CTRL> __device__ __forceinline__ float tr_step(float lo, float hi, bool bit) {
  const float t1 = lo + dppf<CTRL>(lo), t2 = hi + dppf<CTRL>(hi);
  return bit ? t2 : t1;
}
__device__ __forceinline__ float qk4(const v4u& kk, const v4u& q) {
  return dot2(kk.w, q.w, dot2(kk.z, q.z, dot2(kk.y, q.y, dot2(kk.x, q.x, 0.f))));
}
__device__ __forceinline__ float score_round(float x) { return rbf(rbf(x) * 0.08838834764831845f); }

struct TrAcc {
  float l0, l1, l2, l3;
  template <int A> __device__ __forceinline__ void push(float x, int c) {
    if (!(A & 1)) { l0 = x; return; }
    x = tr_step<XOR8>(l0, x, c & 8);
    if (!(A & 2)) { l1 = x; return; }
    x = tr_step<XOR7>(l1, x, c & 4);
    if (!(A & 4)) { l2 = x; return; }
    x = tr_step<XOR2>(l2, x, c & 2);
    if (!(A & 8)) { l3 = x; return; }
    l0 = tr_step<XOR1>(l3, x, c & 1);
  }
};
template <int I, int N, class F> __device__ __forceinline__ void sfor(F&& f) {
  if constexpr (I < N) { f(std::integral_constant<int, I>{}); sfor<I + 1, N>(f); }
}

typedef unsigned long long u64;
#define PL(i, op) "s_mov_b64 exec, %[m" #i "]\n\t" op " %[g" #i "], %[vo], %[rs], %[s" #i "] offen sc1\n\t"
#define PEND "s_mov_b64 exec, -1\n\ts_waitcnt vmcnt(0)"
#define PG(i) [g##i] "+v"(g[i])
#define PI(i) [m##i] "s"(m[i]), [s##i] "s"(so[i])
__device__ __forceinline__ void masked_ld(v2u (&g)[1], const u64 (&m)[1], int vo, rsrc_t rs, const int (&so)[1]) {
  asm volatile("s_nop 4\n\t" PL(0, "buffer_load_dwordx2") PEND : PG(0) : PI(0), [vo] "v"(vo), [rs] "s"(rs) : "memory");
}
__device__ __forceinline__ void masked_ld(v4u (&g)[2], const u64 (&m)[2], int vo, rsrc_t rs, const int (&so)[2]) {
  asm volatile("s_nop 4\n\t" PL(0, "buffer_load_dwordx4") PL(1, "buffer_load_dwordx4") PEND : PG(0), PG(1) : PI(0), PI(1), [vo] "v"(vo), [rs] "s"(rs) : "memory");
}
__device__ __forceinline__ void masked_ld(v4u (&g)[4], const u64 (&m)[4], int vo, rsrc_t rs, const int (&so)[4]) {
  asm volatile("s_nop 4\n\t" PL(0, "buffer_load_dwordx4") PL(1, "buffer_load_dwordx4") PL(2, "buffer_load_dwordx4") PL(3, "buffer_load_dwordx4") PEND
               : PG(0), PG(1), PG(2), PG(3) : PI(0), PI(1), PI(2), PI(3), [vo] "v"(vo), [rs] "s"(rs) : "memory");
}
__device__ __forceinline__ void masked_ld(unsigned (&g)[5], const u64 (&m)[5], int vo, rsrc_t rs, const int (&so)[5]) {
  asm volatile("s_nop 4\n\t" PL(0, "buffer_load_dword") PL(1, "buffer_load_dword") PL(2, "buffer_load_dword") PL(3, "buffer_load_dword") PL(4, "buffer_load_dword") PEND
               : PG(0), PG(1), PG(2), PG(3), PG(4) : PI(0), PI(1), PI(2), PI(3), PI(4), [vo] "v"(vo), [rs] "s"(rs) : "memory");
}
__device__ __forceinline__ unsigned umax(unsigned x) { return x; }
__device__ __forceinline__ unsigned umax(const v2u& x) { return max(x.x, x.y); }
__device__ __forceinline__ unsigned umax(const v4u& x) { return max(max(x.x, x.y), max(x.z, x.w)); }
template <int SLP, int N, class T>
__device__ __forceinline__ void poll_full(T (&g)[N], int vo, rsrc_t rs, int stride) {
  u64 m[N];
  int so[N];
#pragma unroll
  for (int i = 0; i < N; ++i) { m[i] = ~0ull; so[i] = i * stride; g[i] = T{}; }
#pragma nounroll
  for (unsigned spin = 0; spin <= SPIN_CAP; ++spin) {
    masked_ld(g, m, vo, rs, so);
    u64 any = 0;
#pragma unroll
    for (int i = 0; i < N; ++i) { m[i] = __builtin_amdgcn_ballot_w64(umax(g[i]) == EMPTY); any |= m[i]; }
    if (any == 0) break;
    if (SLP) __builtin_amdgcn_s_sleep(SLP);
  }
}
__device__ __forceinline__ unsigned nonempty(unsigned u) { return u == EMPTY ? 0x7fc07fc0u : u; }
__device__ __forceinline__ void helper_sync(LAS unsigned char* L, int lane, unsigned target) {
  LDS_WAIT();
  LAS unsigned* cnt = (LAS unsigned*)(L + L_FL + 4);
  if (lane == 0) __hip_atomic_fetch_add(cnt, 1u, __ATOMIC_RELAXED, __HIP_MEMORY_SCOPE_WORKGROUP);
  for (unsigned spin = 0; *(volatile LAS unsigned*)cnt < target && spin < SPIN_CAP; ++spin) __builtin_amdgcn_s_sleep(1);
  asm volatile("" ::: "memory");
}
__device__ __forceinline__ void axpy8(float (&acc)[8], const v4u& vv, unsigned plo, unsigned phi) {
  acc[0] = dot2(vv.x, plo, acc[0]); acc[1] = dot2(vv.x, phi, acc[1]);
  acc[2] = dot2(vv.y, plo, acc[2]); acc[3] = dot2(vv.y, phi, acc[3]);
  acc[4] = dot2(vv.z, plo, acc[4]); acc[5] = dot2(vv.z, phi, acc[5]);
  acc[6] = dot2(vv.w, plo, acc[6]); acc[7] = dot2(vv.w, phi, acc[7]);
}
__device__ __forceinline__ f32x2 fold_rows(const float (&acc)[8]) {
  const float y0 = swap32_add(acc[0], acc[4]), y1 = swap32_add(acc[1], acc[5]), y2 = swap32_add(acc[2], acc[6]), y3 = swap32_add(acc[3], acc[7]);
  return f32x2{swap16_add(y0, y2), swap16_add(y1, y3)};
}
__device__ __forceinline__ void sum4_to_xb(LAS unsigned char* L, int off, int wave, int lane) {
  f32x2 s = *(LAS f32x2*)(L + off + lane * 8);
#pragma unroll
  for (int i = 1; i < 4; ++i) s += *(LAS f32x2*)(L + off + i * 512 + lane * 8);
  *(LAS unsigned*)(L + L_XB + wave * 256 + lane * 4) = pkbf(s.x, s.y);
  LDS_WAIT();
}

__global__ void __launch_bounds__(NTHR, 2) fused_block(Params p) {
  __shared__ __attribute__((aligned(16))) unsigned char smem[L_END];
  LAS unsigned char* L = (LAS unsigned char*)smem;
  const int tid = threadIdx.x, lane = tid & 63, wave = __builtin_amdgcn_readfirstlane(tid >> 6);
  const int r = lane >> 4, c = lane & 15;
  const int xc = blockIdx.x & 7, sl = blockIdx.x >> 3;
  const int h = blockIdx.x & 31, b = blockIdx.x >> 5;
  const int g2 = xc + 8 * ((sl & 2) + 1 - (sl >> 4)), s8 = ((sl >> 1) & 6) | (sl & 1);
  const bool is_t2 = g2 < 27;
  const int j2 = 8 * g2 + s8;
  const int t3 = 8 * (xc - 3) + s8;
  asm volatile("" ::"s"(p.k), "s"(p.v), "s"(p.x), "s"(p.wq), "s"(p.ws));
  const rsrc_t rW = mkrsrc(p.ws, WS_END);
  volatile LAS unsigned* FL = (volatile LAS unsigned*)(L + L_FL);
  const int hw = wave - 4;
  unsigned tag;
  v4u wb[40];
  STAMP(0);

  if (__builtin_expect(wave < 4, 1)) {
    const size_t kvoff = (size_t)blockIdx.x * (SK * DH);
    const rsrc_t rK = mkrsrc(p.k + kvoff, SK * DH * 2);
    const rsrc_t rV = mkrsrc(p.v + kvoff, SK * DH * 2);
    const int voff = lane * 16 + wave * 4096;
    const int xe = (__builtin_amdgcn_s_getreg(20 | (3 << 11)) & 3u) ^ 3u;
    v4u xb[16 * NXG], buf[8];
    sfor<0, 16 * NXG + 8>([&](auto T) { constexpr int t = T;
      if (t >= THR_D) THROTTLE();
      if (t == 6) STAMP(8);
      if (t == 24) STAMP(9);
      if (t == 40) STAMP(10);
      if constexpr (t < 16 * NXG) xb[t] = ld16<AUX_NT>(rK, voff + (t & 3) * 1024, (t >> 2) * 16384);
      else buf[t & 7] = ld16<AUX_NT>(rK, voff + (t & 3) * 1024, (t >> 2) * 16384);
      SB(); });
    STAMP(7);
    BARRIER();
    STAMP(1);
    tag = FL[0];
    sum4_to_xb(L, L_QP, wave, lane);
    const v4u q = *(LAS v4u*)(L + L_XB + wave * 256 + c * 16);
    float s[16];
    auto kgroup = [&](auto FIRST, int g) -> float {
      const bool last = g == 15;
      const rsrc_t rN = last ? rV : rK;
      const int so0 = g * 65536 + 32768, so1 = last ? 0 : (g + 1) * 65536;
      TrAcc ta, tb[NXG];
      sfor<0, 16>([&](auto T) { constexpr int t = T;
        const float x = qk4(buf[t & 7], q);
        SB();
        if (t >= 8 + THR_D) THROTTLE_IF(g); else if (XM >> t & 1) THROTTLE_X(xe); else THROTTLE();
        if (t < 8) buf[t & 7] = ld16<AUX_NT>(rK, voff + (t & 3) * 1024, so0 + (t >> 2) * 16384);
        else buf[t & 7] = ld16<AUX_NT>(rN, voff + (t & 3) * 1024, so1 + ((t - 8) >> 2) * 16384);
        SB();
        ta.push<t>(x, c);
        if constexpr (FIRST) {
#pragma unroll
          for (int e = 0; e < NXG; ++e) tb[e].template push<t>(qk4(xb[16 * e + t], q), c);
        } });
      if constexpr (FIRST) {
#pragma unroll
        for (int e = 0; e < NXG; ++e) s[15 - NXG + e] = score_round(tb[e].l0);
      }
      return score_round(ta.l0);
    };
#pragma unroll
    for (int i = 0; i < 15 - NXG; ++i) s[i] = 0.f;
    s[15] = kgroup(std::true_type{}, NXG);
#pragma unroll 1
    for (int g = NXG + 1; g < 16; ++g) {
      const float sn = kgroup(std::false_type{}, g);
#pragma unroll
      for (int i = 0; i < 15; ++i) s[i] = s[i + 1];
      s[15] = sn;
    }
    STAMP(2);
    constexpr float LE = 1.4426950408889634f;
    float m = s[0];
#pragma unroll
    for (int i = 1; i < 16; ++i) m = fmaxf(m, s[i]);
    m = wave_max(m);
    float l = 0.f;
    unsigned pp[16];
#pragma unroll
    for (int i = 0; i < 16; ++i) {
      const float e = __builtin_amdgcn_exp2f((s[i] - m) * LE);
      l += e;
      pp[i] = (unsigned)__builtin_bit_cast(u16, (__bf16)e);
    }
    l = wave_sum(l);
    if (lane == 0) {
      *(volatile LAS f32x2*)(L + L_FL + 128 + wave * 8) = f32x2{m, l};
      LDS_WAIT();
      FL[16 + wave] = 1u;
    }
    float acc[8];
#pragma unroll
    for (int i = 0; i < 8; ++i) acc[i] = 0.f;
    float scale = 0.f;
#pragma unroll 1
    for (int g = 0; g < 16; ++g) {
      const int so0 = g * 65536 + 32768, so1 = (g + 1) * 65536;
      const unsigned pc = pp[0];
#pragma unroll
      for (int i = 0; i < 15; ++i) pp[i] = pp[i + 1];
      if (g == 15) {
        for (unsigned spin = 0; (FL[16] & FL[17] & FL[18] & FL[19]) == 0u && spin < SPIN_CAP; ++spin) __builtin_amdgcn_s_sleep(1);
        const f32x4 s01 = *(volatile LAS f32x4*)(L + L_FL + 128), s23 = *(volatile LAS f32x4*)(L + L_FL + 144);
        const float mg = fmaxf(fmaxf(s01.x, s01.z), fmaxf(s23.x, s23.z));
        const float lg = s01.y * __builtin_amdgcn_exp2f((s01.x - mg) * LE) + s01.w * __builtin_amdgcn_exp2f((s01.z - mg) * LE) +
                         s23.y * __builtin_amdgcn_exp2f((s23.x - mg) * LE) + s23.w * __builtin_amdgcn_exp2f((s23.z - mg) * LE);
        scale = __builtin_amdgcn_exp2f((m - mg) * LE) / lg;
      }
      sfor<0, 16>([&](auto T) { constexpr int t = T;
        const unsigned plo = dppu<0x150 + brev4(t)>(pc);
        axpy8(acc, buf[t & 7], plo, plo << 16);
        SB();
        if (XM >> t & 1) THROTTLE_X(xe); else THROTTLE_IF(g);
        if (t < 8) buf[t & 7] = ld16<AUX_NT>(rV, voff + (t & 3) * 1024, so0 + (t >> 2) * 16384);
        else buf[t & 7] = ld16<AUX_NT>(rV, voff + (t & 3) * 1024, so1 + ((t - 8) >> 2) * 16384);
        SB(); });
    }
    *(LAS f32x2*)(L + L_OP + wave * 512 + (8 * c + 2 * r) * 4) = fold_rows(acc) * scale;
    STAMP(3);
  } else {
    {
      unsigned xr[5];
#pragma unroll
      for (int i = 0; i < 5; ++i) xr[i] = ((const unsigned*)p.x)[b * 320 + lane + 64 * i];
      const unsigned epoch = __builtin_amdgcn_raw_buffer_load_b32(rW, 0, 0, 0);
      const rsrc_t rQ = mkrsrc(p.wq, DM * 4096 * 2);
      const int qoff = (160 * hw + 40 * r) * 8192 + h * 256 + c * 16;
      v4u rq[8];
#pragma unroll
      for (int t = 0; t < 8; ++t) rq[t] = ld16<0>(rQ, qoff, t * 8192);
      SB();
      tag = epoch + 1u;
      if (hw == 0) {
        if (lane < 24) FL[lane] = lane == 0 ? tag : 0u;
#pragma unroll
        for (int i = 0; i < 5; ++i) *(LAS unsigned*)(L + L_XR + (lane + 64 * i) * 4) = xr[i];
      }
      layernorm5(xr);
      STAMP(8);
      LAS unsigned char* xp = L + L_HN + hw * HN_STRIDE;
#pragma unroll
      for (int i = 0; i < 5; ++i) *(LAS unsigned*)(xp + (lane + 64 * i) * 4) = xr[i];
      LDS_WAIT();
      float acc[8];
#pragma unroll
      for (int i = 0; i < 8; ++i) acc[i] = 0.f;
#pragma unroll 1
      for (int it = 0; it < 5; ++it) {
        const v4u xn = *(LAS v4u*)(xp + (160 * hw + 40 * r + 8 * it) * 2);
        const int so = it < 4 ? (8 * it + 8) * 8192 : DM * 8192;
        sfor<0, 8>([&](auto T) { constexpr int t = T;
          const unsigned u = xn[t >> 1];
          axpy8(acc, rq[t], (t & 1) ? u >> 16 : u & 0xffffu, (t & 1) ? u & 0xffff0000u : u << 16);
          SB();
          asm volatile("s_waitcnt vmcnt(%0)" ::"n"(QD - 1));
          rq[t] = ld16<0>(rQ, qoff, so + t * 8192);
          SB(); });
      }
      *(LAS f32x2*)(L + L_QP + hw * 512 + (8 * c + 2 * r) * 4) = fold_rows(acc);
    }
    STAMP(7);
    BARRIER();
    STAMP(1);
    const int mk = PFX ? ((xc & 3) == 3 && is_t2 ? (xc >> 2) + 2 * ((g2 >> 3) * 8 + s8) : 40) : (is_t2 ? 40 : t3);
    if (mk < 40 && hw == 3) {
      constexpr int PER = 17 * 1024;
      static_assert(40 * PER >= WS_END - WS_P, "");
#pragma unroll 1
      for (int o = lane * 16; o < PER; o += 1024) __builtin_amdgcn_raw_buffer_store_b128(v4u{EMPTY, EMPTY, EMPTY, EMPTY}, rW, WS_P + mk * PER + o, 0, AUX_SC1);
      asm volatile("s_waitcnt vmcnt(0)" ::: "memory");
      if (lane == 0) __builtin_amdgcn_raw_buffer_store_b64(v2u{tag, ~tag}, rW, WS_F + mk * 8, 0, AUX_SC1);
    }
    {
      const rsrc_t rO = mkrsrc(p.wo + (size_t)h * DH * DM, DH * DM * 2);
      sfor<0, 4>([&](auto KS) { constexpr int ks = KS;
        LAS unsigned char* st = L + ((ks & 1) ? L_ST : L_FW);
#pragma unroll 1
        for (int i = 0; i < 5; ++i) {
          const int so = (32 * ks + 8 * hw) * 1280 + i * 2048;
          const v4u t0 = ld16<0>(rO, lane * 16, so), t1 = ld16<0>(rO, lane * 16 + 1024, so);
          *(LAS v4u*)(st + hw * 10240 + i * 2048 + lane * 16) = t0;
          *(LAS v4u*)(st + hw * 10240 + i * 2048 + 1024 + lane * 16) = t1;
        }
        helper_sync(L, lane, 4 * (ks + 1));
        LAS unsigned char* rp = st + (8 * r + (c >> 2)) * 1280 + hw * 320 + (c & 3) * 8;
#pragma unroll
        for (int tt = 0; tt < 10; ++tt) wb[ks * 10 + tt] = tr_frag(rp + tt * 32, 4 * 1280);
      });
    }
    STAMP(2);
    if (is_t2) {
      if (hw == 0) {
        const rsrc_t rG = mkrsrc(p.wg, DM * FF * 2), rU = mkrsrc(p.wu, DM * FF * 2);
#pragma unroll 1
        for (int i = 0; i < 20; ++i) {
          const int row = (i >> 1) * 64 + lane;
          const v4u t = ld16<0>((i & 1) ? rU : rG, row * (FF * 2) + j2 * 16, 0);
          LAS unsigned* d = (LAS unsigned*)(L + L_FW + row * 32 + (i & 1) * 4);
          d[0] = t.x; d[2] = t.y; d[4] = t.z; d[6] = t.w;
        }
      }
    } else if (hw < 2) {
      const rsrc_t rD = mkrsrc(p.wd, FF * DM * 2);
#pragma unroll 1
      for (int i = hw * 64 + lane; i < T3E * 128; i += 128) *(LAS v4u*)(L + L_FW + i * 16) = ld16<0>(rD, (i >> 1) * (DM * 2) + t3 * 32 + (i & 1) * 16, 0);
    }
    if (hw == 0) {
#pragma nounroll
      for (unsigned spin = 0; spin <= SPIN_CAP; ++spin) {
        const v2u f = __builtin_amdgcn_raw_buffer_load_b64(rW, WS_F + min(lane, 39) * 8, 0, AUX_SC1);
        if (__all(f.x == tag && f.y == ~tag)) break;
        asm volatile("s_sleep 64" ::: "memory");
      }
    }
    STAMP(3);
  }
  int pb = WS_P + (blockIdx.x * DM + 160 * hw + lane) * 4, pb2 = lane < 32 ? pb + 512 : OOB;
  int rdoff = WS_P + (b * NHEAD + 8 * wave + r) * (DM * 4) + h * 256 + c * 16;
  int hoff = lane < 32 ? WS_H + b * (DM * 2) + h * 128 + lane * 4 : OOB;
  PIN(pb); PIN(pb2); PIN(rdoff); PIN(hoff);
  BARRIER();
  STAMP(4);
  if (wave >= 4) {
    sum4_to_xb(L, L_OP, wave, lane);
    v4u oa[4];
#pragma unroll
    for (int ks = 0; ks < 4; ++ks) oa[ks] = *(LAS v4u*)(L + L_XB + wave * 256 + ks * 64 + r * 16);
    float d[10];
#pragma unroll
    for (int tt = 0; tt < 10; ++tt) {
      f32x4 a = {0.f, 0.f, 0.f, 0.f};
#pragma unroll
      for (int ks = 0; ks < 4; ++ks) a = mfma16(oa[ks], wb[ks * 10 + tt], a);
      d[tt] = a.x;
    }
    const bool r1 = r & 1, r2 = r & 2;
    const float l0 = r1 ? d[1] : d[0], l1 = r1 ? d[3] : d[2], h0 = r1 ? d[5] : d[4], h1 = r1 ? d[7] : d[6];
    const unsigned lo = asu(r2 ? l1 : l0), hi = asu(r2 ? h1 : h0), ex = asu(r1 ? d[9] : d[8]);
    __builtin_amdgcn_raw_buffer_store_b32(nonempty(lo), rW, pb, 0, AUX_SC1);
    __builtin_amdgcn_raw_buffer_store_b32(nonempty(hi), rW, pb + 256, 0, AUX_SC1);
    __builtin_amdgcn_raw_buffer_store_b32(nonempty(ex), rW, pb2, 0, AUX_SC1);
    STAMP(5);
    BARRIER();
  } else if (h < 10) {
    const unsigned xw = *(LAS unsigned*)(L + L_XR + (32 * h + (lane & 31)) * 4);
    v4u g[2];
    poll_full<0>(g, rdoff, rW, 4 * DM * 4);
    STAMP(15);
    const float y = swap32_add(swap16_add(asf(g[0].x) + asf(g[1].x), asf(g[0].y) + asf(g[1].y)),
                               swap16_add(asf(g[0].z) + asf(g[1].z), asf(g[0].w) + asf(g[1].w)));
    *(LAS float*)(L + L_QP + wave * 256 + (4 * c + r) * 4) = y;
    BARRIER();
    if (wave == 0) {
      f32x2 y4[4];
#pragma unroll
      for (int i = 0; i < 4; ++i) y4[i] = *(LAS f32x2*)(L + L_QP + i * 256 + (lane & 31) * 8);
      const f32x2 ys = (y4[0] + y4[1]) + (y4[2] + y4[3]);
      __builtin_amdgcn_raw_buffer_store_b32(nonempty(pkbf(blo(xw) + rbf(ys.x), bhi(xw) + rbf(ys.y))), rW, hoff, 0, AUX_SC1);
      STAMP(5);
    }
  } else {
    BARRIER();
  }
  LAS unsigned char* wp = L + L_FW + (8 * r + (c >> 2)) * 32 + (c & 3) * 8;
  if (is_t2) {
    v4u wf[20];
    if (wave == 3) {
#pragma unroll
      for (int ks = 0; ks < 20; ++ks) wf[ks] = tr_frag(wp + ks * 1024, 128);
    }
    int aoff = lane < 8 ? WS_A + j2 * 128 + lane * 16 : OOB;
    int hnoff = L_HN + (c & 7) * HN_STRIDE + r * 16;
    PIN(aoff); PIN(hnoff);
    unsigned hr[5];
    poll_full<1>(hr, WS_H + wave * (DM * 2) + lane * 4, rW, 256);
    STAMP(6);
    layernorm5(hr);
#pragma unroll
    for (int i = 0; i < 5; ++i) *(LAS unsigned*)(L + L_HN + wave * HN_STRIDE + (lane + 64 * i) * 4) = hr[i];
    BARRIER();
    if (wave == 3) {
      STAMP(11);
      f32x4 d0 = {0.f, 0.f, 0.f, 0.f}, d1 = d0;
      LAS unsigned char* hp = L + hnoff;
      v4u hf[20];
#pragma unroll
      for (int ks = 0; ks < 20; ++ks) hf[ks] = *(LAS v4u*)(hp + ks * 64);
      SB();
#pragma unroll
      for (int ks = 0; ks < 20; ks += 2) {
        d0 = mfma16(wf[ks], hf[ks], d0);
        d1 = mfma16(wf[ks + 1], hf[ks + 1], d1);
      }
      d0 += d1;
      float av[2];
#pragma unroll
      for (int i = 0; i < 2; ++i) {
        const float g = rbf(d0[i]), u = rbf(d0[2 + i]);
        const float y = 0.7978845608028654f * (g + 0.044715f * g * g * g);
        const float th = 1.f - 2.f * __builtin_amdgcn_rcpf(__builtin_amdgcn_exp2f(y * 2.8853900817779268f) + 1.f);
        av[i] = rbf(0.5f * g * (1.f + th)) * u;
      }
      const unsigned aw = nonempty(pkbf(av[0], av[1]));
      const auto s16 = __builtin_amdgcn_permlane16_swap(aw, aw, false, false);
      const auto s02 = __builtin_amdgcn_permlane32_swap(s16[0], s16[0], false, false);
      const auto s13 = __builtin_amdgcn_permlane32_swap(s16[1], s16[1], false, false);
      __builtin_amdgcn_raw_buffer_store_b128(v4u{s02[0], s13[0], s02[1], s13[1]}, rW, aoff, 0, AUX_SC1);
      STAMP(12);
    }
  } else {
    if (T3E < 27) {
      static_assert(T3E >= 19, "");
      const int pr = wave + 16 >= T3E ? wave + 16 : wave + 24;
      if (pr < 27) {
        const rsrc_t rD = mkrsrc(p.wd, FF * DM * 2);
        const int vo = (64 * pr + (lane >> 1)) * (DM * 2) + t3 * 32 + (lane & 1) * 16;
        const v4u t0 = ld16<0>(rD, vo, 0), t1 = ld16<0>(rD, vo, 32 * DM * 2);
        *(LAS v4u*)(L + L_FW + pr * 2048 + lane * 16) = t0;
        *(LAS v4u*)(L + L_FW + pr * 2048 + 1024 + lane * 16) = t1;
        LDS_WAIT();
      }
    }
    v4u wf[8];
#pragma unroll
    for (int i = 0; i < 8; ++i) wf[i] = tr_frag(wp + (2 * (wave + 8 * (i >> 1) < 27 ? wave + 8 * (i >> 1) : wave) + (i & 1)) * 1024, 128);
    int ooff = c < 8 ? (c * DM + 16 * t3 + 4 * r) * 2 : OOB;
    PIN(ooff);
    v2u hres[1];
    if (wave == 3)
      poll_full<1>(hres, WS_H + (c & 7) * (DM * 2) + t3 * 32 + r * 8, rW, 0);
    BARRIER();
    asm volatile("s_sleep 40" ::: "memory");
    const int avo = WS_A + wave * 1024 + (c >> 3) * 512 + r * 128 + (c & 7) * 16;
    v4u g[4];
    poll_full<1>(g, avo, rW, 8192);
    STAMP(13);
    f32x4 a = {0.f, 0.f, 0.f, 0.f}, a2 = a;
#pragma unroll
    for (int i = 0; i < 4; ++i) {
      a = mfma16(wf[2 * i], g[i], a);
      a2 = mfma16(wf[2 * i + 1], g[i], a2);
    }
    *(LAS f32x4*)(L + L_ST + wave * 1024 + lane * 16) = c < 8 ? a : a2;
    BARRIER();
    if (wave == 3) {
      f32x4 pt[8];
#pragma unroll
      for (int i = 0; i < 8; ++i) pt[i] = *(LAS f32x4*)(L + L_ST + i * 1024 + lane * 16);
      SB();
      f32x4 t = ((pt[0] + pt[1]) + (pt[2] + pt[3])) + ((pt[4] + pt[5]) + (pt[6] + pt[7]));
#pragma unroll
      for (int i = 0; i < 4; ++i) t[i] += dppf<XOR8>(t[i]);
      const v2u o = {pkbf(blo(hres[0].x) + rbf(t.x), bhi(hres[0].x) + rbf(t.y)), pkbf(blo(hres[0].y) + rbf(t.z), bhi(hres[0].y) + rbf(t.w))};
      __builtin_amdgcn_raw_buffer_store_b64(o, mkrsrc(p.out, 8 * DM * 2), ooff, 0, 0);
      if (t3 == 0 && lane == 0) __hip_atomic_store((unsigned*)p.ws, tag, __ATOMIC_RELAXED, __HIP_MEMORY_SCOPE_AGENT);
      STAMP(14);
    }
  }
  FLUSH();
}

}

extern "C" void kernel_launch(void* const* d_in, const int* in_sizes, int n_in, void* d_out, int out_size, void* d_ws,
                              size_t ws_size, hipStream_t stream) {
  Params p;
  p.x = (const u16*)d_in[0]; p.k = (const u16*)d_in[1]; p.v = (const u16*)d_in[2];
  p.wq = (const u16*)d_in[3]; p.wo = (const u16*)d_in[4];
  p.wg = (const u16*)d_in[6]; p.wu = (const u16*)d_in[7]; p.wd = (const u16*)d_in[8];
  p.out = (u16*)d_out; p.ws = (unsigned char*)d_ws;
  fused_block<<<256, NTHR, 0, stream>>>(p);
}
```
